```python
import torch

def _softmax(x, axis=-1):
    return torch.softmax(x, dim=axis)
def _gelu(x):
    return torch.nn.functional.gelu(x, approximate="tanh")
def _silu(x):
    return torch.nn.functional.silu(x)
def _sigmoid(x):
    return torch.sigmoid(x)
def _layernorm(x, axis=-1, eps=1e-5):
    m = x.mean(dim=axis, keepdim=True)
    v = x.var(dim=axis, keepdim=True, unbiased=False)
    return (x - m) / torch.sqrt(v + eps)
def _scatter_add(tgt, idx, src, axis=0):
    return tgt.clone().index_add_(axis, idx, src)
def _segment_sum(src, seg, n):
    out = torch.zeros((n,) + src.shape[1:], dtype=src.dtype)
    return out.index_add_(0, seg, src)
def _pad(x, pw):
    flat = tuple(v for pair in reversed(pw) for v in pair)
    return torch.nn.functional.pad(x, flat)

kernel_name = 'recurrent_scan__mha_block_cached__dense_activation_gelu__decode'

def setup_inputs():

    _w = (lambda a: (lambda t: t.to(torch.bfloat16) if t.is_floating_point() else t)(torch.as_tensor(a)))
    def _A_setup():
        B, Sq, D = 8, 1, 640
        H, Sk, Dh = 32, 4096, 128
        return {
            "x": _w((lambda *s: torch.randn(*s, dtype=torch.bfloat16) * 0.1)(B, Sq, D)),
            "k_cache": _w((lambda *s: torch.randn(*s, dtype=torch.bfloat16) * 0.8)(B, H, Sk, Dh)),
            "v_cache": _w((lambda *s: torch.randn(*s, dtype=torch.bfloat16) * 3.2)(B, H, Sk, Dh)),
            "wq": _w((lambda *s: torch.randn(*s, dtype=torch.bfloat16) * 0.8)(D, H * Dh) * (D ** -0.5)),
            "wo": _w((lambda *s: torch.randn(*s, dtype=torch.bfloat16) * 3.2)(H * Dh, D) * ((H * Dh) ** -0.5)),
        }
    _A_in = _A_setup()

    def _B_setup():
        B, S, D, F = 8, 1, 640, 1728
        return {
            "x": _w((lambda *s: torch.randn(*s, dtype=torch.bfloat16) * 0.1)(B, S, D)),
            "w1": _w((lambda *s: torch.randn(*s, dtype=torch.bfloat16) * 3.2)(D, F) * (D ** -0.5)),
            "b1": _w((lambda *s: torch.randn(*s, dtype=torch.bfloat16) * 3.2)(F) * 0.01),
            "w2": _w((lambda *s: torch.randn(*s, dtype=torch.bfloat16) * 3.2)(F, D) * (F ** -0.5)),
            "b2": _w((lambda *s: torch.randn(*s, dtype=torch.bfloat16) * 3.2)(D) * 0.01),
        }
    _B_in = _B_setup()

    return {
        "A_x": _A_in["x"],
        "A_k_cache": _A_in["k_cache"],
        "A_v_cache": _A_in["v_cache"],
        "A_wq": _A_in["wq"],
        "A_wo": _A_in["wo"],
        "B_x": _B_in["x"],
        "B_w1": _B_in["w1"],
        "B_b1": _B_in["b1"],
        "B_w2": _B_in["w2"],
        "B_b2": _B_in["b2"],
    }

def reference(A_x, A_k_cache, A_v_cache, A_wq, A_wo, B_x, B_w1, B_b1, B_w2, B_b2):
    def _A_ref(x, k_cache, v_cache, wq, wo):
        B, Sq, D = x.shape
        H, Dh = 32, 128
        q = (lambda x, *d: x.permute(*d))((x @ wq).reshape(B, Sq, H, Dh), 0, 2, 1, 3)
        scores = torch.einsum("bhqd,bhkd->bhqk", q, k_cache) * (Dh ** -0.5)
        p = _softmax(scores, axis=-1)
        o = torch.einsum("bhqk,bhkd->bhqd", p, v_cache)
        return (lambda x, *d: x.permute(*d))(o, 0, 2, 1, 3).reshape(B, Sq, H * Dh) @ wo

    def _B_ref(x, w1, b1, w2, b2):
        h = torch.einsum("bsd,df->bsf", x, w1) + b1
        h = _gelu(h)
        return torch.einsum("bsf,fd->bsd", h, w2) + b2

    x = A_x
    xn = _layernorm(x, axis=-1)
    s = _A_ref(xn, A_k_cache, A_v_cache, A_wq, A_wo)
    g = _silu(_B_ref(xn, B_w1, B_b1, B_w2, B_b2))
    return x + s * g
```

```cpp
#include <hip/hip_runtime.h>
#include <hip/hip_bf16.h>
#include <stdint.h>
#define STAMP_OFF (64ull << 20)
#define TS(i)

namespace {
constexpr int NB = 8, DM = 640, NH = 32, SK = 4096, DH = 128, FF = 1728;
constexpr int NTHR = 1024, NW = 16;
constexpr int NFS = 27;
constexpr int ST_B = NW * 4096;
constexpr int NST = SK * DH * 2 / ST_B;

using u32x4 = __attribute__((ext_vector_type(4))) unsigned;
using f32x4 = __attribute__((ext_vector_type(4))) float;
using f32x2 = __attribute__((ext_vector_type(2))) float;
using bf16x2 = __attribute__((ext_vector_type(2))) __bf16;

__device__ __forceinline__ float bflo(unsigned v) { return __builtin_bit_cast(float, v << 16); }
__device__ __forceinline__ float bfhi(unsigned v) { return __builtin_bit_cast(float, v & 0xffff0000u); }
__device__ __forceinline__ float bfr(float x) { return (float)(__bf16)x; }
__device__ __forceinline__ unsigned short bfbits(float x) { return __builtin_bit_cast(unsigned short, (__bf16)x); }
__device__ __forceinline__ float dot2(unsigned k, unsigned q, float acc) {
  return __builtin_amdgcn_fdot2_f32_bf16(__builtin_bit_cast(bf16x2, k), __builtin_bit_cast(bf16x2, q), acc, false);
}
__device__ __forceinline__ void axpy8(float (&acc)[8], float s, const u32x4& wv) {
  const unsigned hi = __float_as_uint(s), lo = hi >> 16;
#pragma unroll
  for (int k = 0; k < 4; ++k) { acc[2 * k] = dot2(wv[k], lo, acc[2 * k]); acc[2 * k + 1] = dot2(wv[k], hi, acc[2 * k + 1]); }
}
__device__ __forceinline__ void fma8(float (&acc)[8], float s, const u32x4& wv) {
#pragma unroll
  for (int k = 0; k < 4; ++k) { acc[2 * k] = fmaf(s, bflo(wv[k]), acc[2 * k]); acc[2 * k + 1] = fmaf(s, bfhi(wv[k]), acc[2 * k + 1]); }
}
template <int CTRL> __device__ __forceinline__ float dpp(float x) {
  return __builtin_bit_cast(float, __builtin_amdgcn_mov_dpp(__builtin_bit_cast(int, x), CTRL, 0xf, 0xf, true));
}
constexpr int XOR1 = 0xB1, XOR2 = 0x4E, HMIR = 0x141, MIR = 0x140, ROR8 = 0x128;
__device__ __forceinline__ float row16_sum(float x) {
  x += dpp<XOR1>(x); x += dpp<XOR2>(x); x += dpp<HMIR>(x); x += dpp<MIR>(x); return x;
}
__device__ __forceinline__ float xrow_max(float x) {
  auto s = __builtin_amdgcn_permlane16_swap(__float_as_uint(x), __float_as_uint(x), false, false);
  x = fmaxf(__uint_as_float(s[0]), __uint_as_float(s[1]));
  auto t = __builtin_amdgcn_permlane32_swap(__float_as_uint(x), __float_as_uint(x), false, false);
  return fmaxf(__uint_as_float(t[0]), __uint_as_float(t[1]));
}
__device__ __forceinline__ float xrow_sum(float x) {
  auto s = __builtin_amdgcn_permlane16_swap(__float_as_uint(x), __float_as_uint(x), false, false);
  x = __uint_as_float(s[0]) + __uint_as_float(s[1]);
  auto t = __builtin_amdgcn_permlane32_swap(__float_as_uint(x), __float_as_uint(x), false, false);
  return __uint_as_float(t[0]) + __uint_as_float(t[1]);
}
__device__ __forceinline__ float wave_sum(float x) { return xrow_sum(row16_sum(x)); }
__device__ __forceinline__ float max16(const float* p) {
  const f32x4 m0 = *reinterpret_cast<const f32x4*>(p), m1 = *reinterpret_cast<const f32x4*>(p + 4);
  const f32x4 m2 = *reinterpret_cast<const f32x4*>(p + 8), m3 = *reinterpret_cast<const f32x4*>(p + 12);
  return fmaxf(fmaxf(fmaxf(fmaxf(m0[0], m0[1]), fmaxf(m0[2], m0[3])), fmaxf(fmaxf(m1[0], m1[1]), fmaxf(m1[2], m1[3]))),
               fmaxf(fmaxf(fmaxf(m2[0], m2[1]), fmaxf(m2[2], m2[3])), fmaxf(fmaxf(m3[0], m3[1]), fmaxf(m3[2], m3[3]))));
}
__device__ __forceinline__ float sum16(const float* p) {
  const f32x4 m0 = *reinterpret_cast<const f32x4*>(p), m1 = *reinterpret_cast<const f32x4*>(p + 4);
  const f32x4 m2 = *reinterpret_cast<const f32x4*>(p + 8), m3 = *reinterpret_cast<const f32x4*>(p + 12);
  return (((m0[0] + m0[1]) + (m0[2] + m0[3])) + ((m1[0] + m1[1]) + (m1[2] + m1[3]))) + (((m2[0] + m2[1]) + (m2[2] + m2[3])) + ((m3[0] + m3[1]) + (m3[2] + m3[3])));
}

#define LDG(T, base, off) (*reinterpret_cast<const T*>(reinterpret_cast<const char*>(base) + (unsigned)(off)))
#define FENCE() do { asm volatile("" ::: "memory"); __builtin_amdgcn_sched_barrier(0); } while (0)
#define PIN8(a) asm volatile("" : "+v"(a[0]), "+v"(a[1]), "+v"(a[2]), "+v"(a[3]), "+v"(a[4]), "+v"(a[5]), "+v"(a[6]), "+v"(a[7]))
#define MKRSRC(p, bytes) __builtin_amdgcn_make_buffer_rsrc((void*)(p), (short)0, (int)(bytes), 0x00020000)
#define BLD(rs, voff, soff, aux) __builtin_amdgcn_raw_buffer_load_b128((rs), (int)(voff), (int)(soff), (aux))
#define WAUX 0
#define KVAUX 2
#define LOCKSTEP 1

constexpr int RQS = 132;
constexpr int L_XS = 0;
constexpr int L_RQ = L_XS + 640;
constexpr int L_RH = L_RQ + NW * RQS;
constexpr int L_Q = L_RH + NW * 64;
constexpr int L_HV = L_Q + 64;
constexpr int L_OV = L_HV + 80;
constexpr int L_ML = L_OV + 144;
constexpr int L_SC = L_ML + 32;
constexpr int L_RED = L_SC + SK;
constexpr int L_END = L_RED + 13 * 640;

__global__ void __launch_bounds__(NTHR) fused_attn(const unsigned short* x, const unsigned short* kc, const unsigned short* vc,
                                                   const unsigned short* wq, const unsigned short* wo, const unsigned short* w1,
                                                   const unsigned short* b1, const unsigned short* w2, float* sp, float* yp) {
  __shared__ __attribute__((aligned(16))) float lds[L_END];
  const int bid = blockIdx.x, xcd = bid & 7, jj = (bid >> 3) & 3, b = bid >> 5, h = xcd * 4 + jj;
  const int fs_raw = xcd + 8 * jj;
  const bool has_ffn = fs_raw < NFS;
  const int fs = has_ffn ? fs_raw : 0;
  const int tid = threadIdx.x, lane = tid & 63, w = __builtin_amdgcn_readfirstlane(tid >> 6);
  const int g = lane >> 4, c = lane & 15, g8 = lane >> 3, c8 = lane & 7;
  TS(0);

  const auto rsK = MKRSRC(kc + (size_t)(b * NH + h) * (SK * DH), SK * DH * 2);
  const auto rsV = MKRSRC(vc + (size_t)(b * NH + h) * (SK * DH), SK * DH * 2);
  const auto rsO = MKRSRC(wo + (size_t)h * DH * DM, DH * DM * 2);
  const auto rs1 = MKRSRC(w1, has_ffn ? DM * FF * 2 : 0);
  const auto rs2 = MKRSRC(w2 + (size_t)fs * 64 * DM, has_ffn ? 64 * DM * 2 : 0);

  const unsigned loff = lane * 16;
  u32x4 SA[4], SB[4];
#define ISSUE(SX, RS, SOFF) do { _Pragma("unroll") for (int u = 0; u < 4; ++u) SX[u] = BLD(RS, loff + u * 1024, (SOFF), KVAUX); FENCE(); } while (0)
  const int wofs = w * 4096;
  ISSUE(SA, rsK, wofs);

  const int rb = (w + 2 * b) & 15;
  const unsigned short* xb = x + b * DM;
  unsigned xr[5];
#pragma unroll
  for (int j = 0; j < 5; ++j) xr[j] = LDG(unsigned, xb, lane * 4 + 256 * j);
  const unsigned xme = LDG(unsigned short, xb + rb * 40, (lane < 40 ? lane : 0) * 2);
  const unsigned b1v = LDG(unsigned short, b1 + fs * 64, lane * 2);
  FENCE();
  u32x4 wqv[10];
  {
    const auto rs = MKRSRC(wq, DM * NH * DH * 2);
    const unsigned voff = (unsigned)(g * 10) * (NH * DH * 2) + c * 16;
    const unsigned soff = (unsigned)(rb * 40) * (NH * DH * 2) + h * (DH * 2);
#pragma unroll
    for (int i = 0; i < 10; ++i) wqv[i] = BLD(rs, voff, soff + i * (NH * DH * 2), WAUX);
  }
  FENCE();

  float sum = 0.f;
#pragma unroll
  for (int j = 0; j < 5; ++j) sum += bflo(xr[j]) + bfhi(xr[j]);
  sum = wave_sum(sum);
  const float mean = sum * (1.0f / DM);
  float sq = 0.f;
#pragma unroll
  for (int j = 0; j < 5; ++j) { const float a = bflo(xr[j]) - mean, bb = bfhi(xr[j]) - mean; sq += a * a + bb * bb; }
  sq = wave_sum(sq);
  const float mb = bfr(mean), vb = bfr(sq * (1.0f / DM));
  const float den = bfr(sqrtf(bfr(vb + 1e-5f)));
  const float xn = bfr(bfr(bflo(xme) - mb) / den);
  TS(1);
  float* xs = lds + L_XS + rb * 40;
  if (lane < 40) xs[lane] = xn;
  __builtin_amdgcn_s_waitcnt(0xC07F);
  __builtin_amdgcn_wave_barrier();

  {
    float qa[8];
#pragma unroll
    for (int k = 0; k < 8; ++k) qa[k] = 0.f;
#pragma unroll
    for (int i = 0; i < 10; i += 2) {
      const f32x2 xv = *reinterpret_cast<const f32x2*>(xs + g * 10 + i);
      axpy8(qa, xv[0], wqv[i]); axpy8(qa, xv[1], wqv[i + 1]);
    }
#pragma unroll
    for (int k = 0; k < 8; ++k) qa[k] = xrow_sum(qa[k]);
    if (lane < 16) {
      *reinterpret_cast<f32x4*>(lds + L_RQ + rb * RQS + c * 8) = f32x4{qa[0], qa[1], qa[2], qa[3]};
      *reinterpret_cast<f32x4*>(lds + L_RQ + rb * RQS + c * 8 + 4) = f32x4{qa[4], qa[5], qa[6], qa[7]};
    }
  }
  TS(2);
  FENCE();
  ISSUE(SB, rsK, wofs + ST_B);
  __syncthreads();
  if (w < 2) {
    float s = 0.f;
#pragma unroll
    for (int ww = 0; ww < NW; ++ww) s += lds[L_RQ + ww * RQS + tid];
    reinterpret_cast<unsigned short*>(lds + L_Q)[tid] = bfbits(s);
  } else if (w == 3 && lane < 16) {
    lds[L_HV + 64 + lane] = 0.f;
    lds[L_OV + 128 + lane] = 0.f;
  }
  __syncthreads();
  const u32x4 qpk = *reinterpret_cast<const u32x4*>(lds + L_Q + c * 4);
  TS(3);

  float mx = -INFINITY;
  float* scw = lds + L_SC + w * 16 + g * 4;
#define QK(SX, ST) do { \
    float sc_[4]; \
    _Pragma("unroll") for (int u = 0; u < 4; ++u) { float d = dot2(SX[u][0], qpk[0], 0.f); d = dot2(SX[u][1], qpk[1], d); d = dot2(SX[u][2], qpk[2], d); d = dot2(SX[u][3], qpk[3], d); sc_[u] = d; } \
    _Pragma("unroll") for (int u = 0; u < 4; ++u) sc_[u] = bfr(bfr(row16_sum(sc_[u])) * 0.088388347648318440f); \
    mx = fmaxf(fmaxf(fmaxf(sc_[0], sc_[1]), fmaxf(sc_[2], sc_[3])), mx); \
    if (c == 0) *reinterpret_cast<f32x4*>(scw + (ST) * 256) = f32x4{sc_[0], sc_[1], sc_[2], sc_[3]}; \
    asm volatile("" : "+v"(mx)); \
    FENCE(); \
  } while (0)
#define STEPBAR() do { __builtin_amdgcn_s_barrier(); FENCE(); } while (0)
#define MIDBAR() do { } while (0)
#define KIT_A(IT) do { QK(SA, 2 * (IT)); ISSUE(SA, rsK, wofs + (2 * (IT) + 2) * ST_B); } while (0)
#define KIT_B(IT) do { MIDBAR(); QK(SB, 2 * (IT) + 1); ISSUE(SB, rsK, wofs + (2 * (IT) + 3) * ST_B); } while (0)

  const int slot = tid / 80;
  u32x4 w1v[5];
  KIT_A(0);
  {
    const unsigned voff = (unsigned)(g8 * 5) * (FF * 2) + c8 * 16;
    const unsigned soff = (unsigned)(rb * 40) * (FF * 2) + fs * 128;
#pragma unroll
    for (int i = 0; i < 5; ++i) w1v[i] = BLD(rs1, voff, soff + i * (FF * 2), WAUX);
    FENCE();
  }
  KIT_B(0);
  STEPBAR();
  KIT_A(1);
  KIT_B(1);
  {
    float ha[8];
#pragma unroll
    for (int k = 0; k < 8; ++k) ha[k] = 0.f;
#pragma unroll
    for (int i = 0; i < 5; ++i) axpy8(ha, xs[g8 * 5 + i], w1v[i]);
#pragma unroll
    for (int k = 0; k < 8; ++k) { float t = ha[k]; t += dpp<ROR8>(t); ha[k] = xrow_sum(t); }
    if (lane < 8) {
      *reinterpret_cast<f32x4*>(lds + L_RH + rb * 64 + c8 * 8) = f32x4{ha[0], ha[1], ha[2], ha[3]};
      *reinterpret_cast<f32x4*>(lds + L_RH + rb * 64 + c8 * 8 + 4) = f32x4{ha[4], ha[5], ha[6], ha[7]};
    }
  }
  __syncthreads(); FENCE();
  if (w == 0) {
    float s = 0.f;
#pragma unroll
    for (int ww = 0; ww < NW; ++ww) s += lds[L_RH + ww * 64 + lane];
    const float h2 = bfr(bfr(s) + bflo(b1v));
    const float inner = 0.7978845608028654f * (h2 + 0.044715f * h2 * h2 * h2);
    lds[L_HV + lane] = bfr(0.5f * h2 * (1.0f + tanhf(inner)));
  }
  FENCE();
  u32x4 w2v[6];
  KIT_A(2);
#pragma unroll
  for (int i = 0; i < 6; ++i) w2v[i] = BLD(rs2, tid * 16, 960 * 16 * i, WAUX);
  FENCE();
  KIT_B(2);
  __syncthreads(); FENCE();
  KIT_A(3);
  KIT_B(3);
  {
    float ya[8];
#pragma unroll
    for (int k = 0; k < 8; ++k) ya[k] = 0.f;
#pragma unroll
    for (int i = 0; i < 6; ++i) axpy8(ya, lds[L_HV + slot + 12 * i], w2v[i]);
    *reinterpret_cast<f32x4*>(lds + L_RED + tid * 8) = f32x4{ya[0], ya[1], ya[2], ya[3]};
    *reinterpret_cast<f32x4*>(lds + L_RED + tid * 8 + 4) = f32x4{ya[4], ya[5], ya[6], ya[7]};
  }
  __syncthreads(); FENCE();
  float ysum = 0.f;
  if (w < 10) {
#pragma unroll
    for (int sl = 0; sl < 12; ++sl) ysum += lds[L_RED + sl * DM + tid];
  }
  FENCE();
  u32x4 wov[11];
#define KIT_WO(IT, W0, W1) do { KIT_A(IT); \
    _Pragma("unroll") for (int i = (W0); i < (W1); ++i) wov[i] = BLD(rsO, tid * 16, 960 * 16 * i, WAUX); \
    FENCE(); KIT_B(IT); STEPBAR(); } while (0)
  KIT_WO(4, 0, 4);
  KIT_WO(5, 4, 8);
  KIT_WO(6, 8, 11);
  QK(SA, 14); ISSUE(SA, rsV, wofs);
  QK(SB, 15); ISSUE(SB, rsV, wofs + ST_B);
  TS(4);

  mx = xrow_max(mx);
  if (lane == 0) lds[L_ML + w] = mx;
  __syncthreads();
  {
    const float M = max16(lds + L_ML);
    f32x4 e = *reinterpret_cast<const f32x4*>(lds + L_SC + tid * 4);
#pragma unroll
    for (int k = 0; k < 4; ++k) e[k] = __builtin_amdgcn_exp2f((e[k] - M) * 1.4426950408889634f);
    const float ps = wave_sum((e[0] + e[1]) + (e[2] + e[3]));
    if (lane == 0) lds[L_ML + NW + w] = ps;
    __syncthreads();
    const float inv = 1.0f / sum16(lds + L_ML + NW);
#pragma unroll
    for (int k = 0; k < 4; ++k) e[k] = bfr(e[k] * inv);
    *reinterpret_cast<f32x4*>(lds + L_SC + tid * 4) = e;
  }
  __syncthreads(); FENCE();

  float acc[8];
#pragma unroll
  for (int k = 0; k < 8; ++k) acc[k] = 0.f;
#define PV(SX, ST) do { \
    const f32x4 p_ = *reinterpret_cast<const f32x4*>(scw + (ST) * 256); \
    _Pragma("unroll") for (int u = 0; u < 4; ++u) fma8(acc, p_[u], SX[u]); \
    PIN8(acc); FENCE(); \
  } while (0)
#pragma unroll 1
  for (int it = 0; it < NST / 2 - 1; ++it) {
    PV(SA, 2 * it); ISSUE(SA, rsV, wofs + (2 * it + 2) * ST_B);
    MIDBAR();
    PV(SB, 2 * it + 1); ISSUE(SB, rsV, wofs + (2 * it + 3) * ST_B);
    STEPBAR();
  }
  PV(SA, NST - 2);
  PV(SB, NST - 1);
  TS(5);

#pragma unroll
  for (int k = 0; k < 8; ++k) acc[k] = xrow_sum(acc[k]);
  if (lane < 16) {
    *reinterpret_cast<f32x4*>(lds + L_RQ + w * RQS + c * 8) = f32x4{acc[0], acc[1], acc[2], acc[3]};
    *reinterpret_cast<f32x4*>(lds + L_RQ + w * RQS + c * 8 + 4) = f32x4{acc[4], acc[5], acc[6], acc[7]};
  }
  __syncthreads();
  {
    const int d = w * 8 + c8;
    float o = lds[L_RQ + (2 * g8) * RQS + d] + lds[L_RQ + (2 * g8 + 1) * RQS + d];
    o += dpp<ROR8>(o);
    o = xrow_sum(o);
    if (lane < 8) lds[L_OV + d] = bfr(o);
  }
  __syncthreads();
  TS(6);
  {
    float sa[8];
#pragma unroll
    for (int k = 0; k < 8; ++k) sa[k] = 0.f;
    float ov[11];
#pragma unroll
    for (int i = 0; i < 11; ++i) ov[i] = lds[L_OV + slot + 12 * i];
    __builtin_amdgcn_sched_barrier(0);
#pragma unroll
    for (int i = 0; i < 11; ++i) axpy8(sa, ov[i], wov[i]);
    *reinterpret_cast<f32x4*>(lds + L_RED + tid * 8) = f32x4{sa[0], sa[1], sa[2], sa[3]};
    *reinterpret_cast<f32x4*>(lds + L_RED + tid * 8 + 4) = f32x4{sa[4], sa[5], sa[6], sa[7]};
    __syncthreads();
    if (w < 10) {
      float s = 0.f;
#pragma unroll
      for (int sl = 0; sl < 12; ++sl) s += lds[L_RED + sl * DM + tid];
      sp[(size_t)(b * NH + h) * DM + tid] = s;
      if (has_ffn) yp[(size_t)(b * NFS + fs) * DM + tid] = ysum;
    }
  }
}

__global__ void __launch_bounds__(256) final_k(const unsigned short* x, const unsigned short* b2, const float* sp,
                                               const float* yp, unsigned short* out) {
  __shared__ float red[2][4][64];
  const int b = blockIdx.x / 10, cb = blockIdx.x % 10;
  const int lane = threadIdx.x & 63, w = threadIdx.x >> 6;
  const int col = cb * 64 + lane;
  float s = 0.f, y = 0.f;
  float sv[8], yv[7];
  const unsigned xv = x[b * DM + col], b2v = b2[col];
#pragma unroll
  for (int i = 0; i < 8; ++i) sv[i] = sp[(size_t)(b * NH + w * 8 + i) * DM + col];
#pragma unroll
  for (int i = 0; i < 7; ++i) { const int f = w * 7 + i; yv[i] = yp[(size_t)(b * NFS + (f < NFS ? f : 0)) * DM + col]; }
  FENCE();
  if (w == 3) yv[6] = 0.f;
#pragma unroll
  for (int i = 0; i < 8; ++i) s += sv[i];
#pragma unroll
  for (int i = 0; i < 7; ++i) y += yv[i];
  red[0][w][lane] = s; red[1][w][lane] = y;
  __syncthreads();
  if (w == 0) {
    s = (red[0][0][lane] + red[0][1][lane]) + (red[0][2][lane] + red[0][3][lane]);
    y = (red[1][0][lane] + red[1][1][lane]) + (red[1][2][lane] + red[1][3][lane]);
    const float sb = bfr(s);
    const float yb = bfr(bfr(y) + bflo(b2v));
    const float gt = bfr(yb / (1.0f + __expf(-yb)));
    const float o = bflo(xv) + bfr(sb * gt);
    out[b * DM + col] = bfbits(o);
  }
}
}

extern "C" void kernel_launch(void* const* d_in, const int* in_sizes, int n_in, void* d_out, int out_size, void* d_ws, size_t ws_size,
                              hipStream_t stream) {
  const unsigned short* x = (const unsigned short*)d_in[0];
  const unsigned short* kc = (const unsigned short*)d_in[1];
  const unsigned short* vc = (const unsigned short*)d_in[2];
  const unsigned short* wq = (const unsigned short*)d_in[3];
  const unsigned short* wo = (const unsigned short*)d_in[4];
  const unsigned short* w1 = (const unsigned short*)d_in[6];
  const unsigned short* b1 = (const unsigned short*)d_in[7];
  const unsigned short* w2 = (const unsigned short*)d_in[8];
  const unsigned short* b2 = (const unsigned short*)d_in[9];
  float* sp = (float*)d_ws;
  float* yp = sp + NB * NH * DM;
  fused_attn<<<NB * NH, NTHR, 0, stream>>>(x, kc, vc, wq, wo, w1, b1, w2, sp, yp);
  final_k<<<NB * 10, 256, 0, stream>>>(x, b2, sp, yp, (unsigned short*)d_out);
}
```

```cpp
#include <hip/hip_runtime.h>
#include <hip/hip_bf16.h>
#include <stdint.h>

namespace {
constexpr int NB = 8, DM = 640, NH = 32, SK = 4096, DH = 128, FF = 1728;
constexpr int NTHR = 1024, NW = 16;
constexpr int NFS = 27;
constexpr int ST_B = NW * 4096;
constexpr int NST = SK * DH * 2 / ST_B;

using u32x4 = __attribute__((ext_vector_type(4))) unsigned;
using f32x4 = __attribute__((ext_vector_type(4))) float;
using f32x2 = __attribute__((ext_vector_type(2))) float;
using bf16x2 = __attribute__((ext_vector_type(2))) __bf16;

__device__ __forceinline__ float bflo(unsigned v) { return __builtin_bit_cast(float, v << 16); }
__device__ __forceinline__ float bfhi(unsigned v) { return __builtin_bit_cast(float, v & 0xffff0000u); }
__device__ __forceinline__ float bfr(float x) { return (float)(__bf16)x; }
__device__ __forceinline__ unsigned short bfbits(float x) { return __builtin_bit_cast(unsigned short, (__bf16)x); }
__device__ __forceinline__ float dot2(unsigned k, unsigned q, float acc) {
  return __builtin_amdgcn_fdot2_f32_bf16(__builtin_bit_cast(bf16x2, k), __builtin_bit_cast(bf16x2, q), acc, false);
}
__device__ __forceinline__ void axpy8(float (&acc)[8], float s, const u32x4& wv) {
  const unsigned hi = __float_as_uint(s), lo = hi >> 16;
#pragma unroll
  for (int k = 0; k < 4; ++k) { acc[2 * k] = dot2(wv[k], lo, acc[2 * k]); acc[2 * k + 1] = dot2(wv[k], hi, acc[2 * k + 1]); }
}
__device__ __forceinline__ void fma8(float (&acc)[8], float s, const u32x4& wv) {
#pragma unroll
  for (int k = 0; k < 4; ++k) { acc[2 * k] = fmaf(s, bflo(wv[k]), acc[2 * k]); acc[2 * k + 1] = fmaf(s, bfhi(wv[k]), acc[2 * k + 1]); }
}
template <int CTRL> __device__ __forceinline__ float dpp(float x) {
  return __builtin_bit_cast(float, __builtin_amdgcn_mov_dpp(__builtin_bit_cast(int, x), CTRL, 0xf, 0xf, true));
}
constexpr int XOR1 = 0xB1, XOR2 = 0x4E, HMIR = 0x141, MIR = 0x140, ROR8 = 0x128;
__device__ __forceinline__ float row16_sum(float x) {
  x += dpp<XOR1>(x); x += dpp<XOR2>(x); x += dpp<HMIR>(x); x += dpp<MIR>(x); return x;
}
__device__ __forceinline__ float xrow_max(float x) {
  auto s = __builtin_amdgcn_permlane16_swap(__float_as_uint(x), __float_as_uint(x), false, false);
  x = fmaxf(__uint_as_float(s[0]), __uint_as_float(s[1]));
  auto t = __builtin_amdgcn_permlane32_swap(__float_as_uint(x), __float_as_uint(x), false, false);
  return fmaxf(__uint_as_float(t[0]), __uint_as_float(t[1]));
}
__device__ __forceinline__ float xrow_sum(float x) {
  auto s = __builtin_amdgcn_permlane16_swap(__float_as_uint(x), __float_as_uint(x), false, false);
  x = __uint_as_float(s[0]) + __uint_as_float(s[1]);
  auto t = __builtin_amdgcn_permlane32_swap(__float_as_uint(x), __float_as_uint(x), false, false);
  return __uint_as_float(t[0]) + __uint_as_float(t[1]);
}
__device__ __forceinline__ float wave_sum(float x) { return xrow_sum(row16_sum(x)); }
__device__ __forceinline__ float max16(const float* p) {
  const f32x4 m0 = *reinterpret_cast<const f32x4*>(p), m1 = *reinterpret_cast<const f32x4*>(p + 4);
  const f32x4 m2 = *reinterpret_cast<const f32x4*>(p + 8), m3 = *reinterpret_cast<const f32x4*>(p + 12);
  return fmaxf(fmaxf(fmaxf(fmaxf(m0[0], m0[1]), fmaxf(m0[2], m0[3])), fmaxf(fmaxf(m1[0], m1[1]), fmaxf(m1[2], m1[3]))),
               fmaxf(fmaxf(fmaxf(m2[0], m2[1]), fmaxf(m2[2], m2[3])), fmaxf(fmaxf(m3[0], m3[1]), fmaxf(m3[2], m3[3]))));
}
__device__ __forceinline__ float sum16(const float* p) {
  const f32x4 m0 = *reinterpret_cast<const f32x4*>(p), m1 = *reinterpret_cast<const f32x4*>(p + 4);
  const f32x4 m2 = *reinterpret_cast<const f32x4*>(p + 8), m3 = *reinterpret_cast<const f32x4*>(p + 12);
  return (((m0[0] + m0[1]) + (m0[2] + m0[3])) + ((m1[0] + m1[1]) + (m1[2] + m1[3]))) + (((m2[0] + m2[1]) + (m2[2] + m2[3])) + ((m3[0] + m3[1]) + (m3[2] + m3[3])));
}

#define LDG(T, base, off) (*reinterpret_cast<const T*>(reinterpret_cast<const char*>(base) + (unsigned)(off)))
#define FENCE() do { asm volatile("" ::: "memory"); __builtin_amdgcn_sched_barrier(0); } while (0)
#define PIN8(a) asm volatile("" : "+v"(a[0]), "+v"(a[1]), "+v"(a[2]), "+v"(a[3]), "+v"(a[4]), "+v"(a[5]), "+v"(a[6]), "+v"(a[7]))
#define MKRSRC(p, bytes) __builtin_amdgcn_make_buffer_rsrc((void*)(p), (short)0, (int)(bytes), 0x00020000)
#define BLD(rs, voff, soff, aux) __builtin_amdgcn_raw_buffer_load_b128((rs), (int)(voff), (int)(soff), (aux))
#define WAUX 0
#define KVAUX 2

constexpr int RQS = 132;
constexpr int L_XS = 0;
constexpr int L_RQ = L_XS + 640;
constexpr int L_RH = L_RQ + NW * RQS;
constexpr int L_Q = L_RH + NW * 64;
constexpr int L_HV = L_Q + 64;
constexpr int L_OV = L_HV + 80;
constexpr int L_ML = L_OV + 144;
constexpr int L_SC = L_ML + 32;
constexpr int L_RED = L_SC + SK;
constexpr int L_END = L_RED + 13 * 640;

__global__ void __launch_bounds__(NTHR) fused_attn(const unsigned short* x, const unsigned short* kc, const unsigned short* vc,
                                                   const unsigned short* wq, const unsigned short* wo, const unsigned short* w1,
                                                   const unsigned short* b1, const unsigned short* w2, float* sp, float* yp) {
  __shared__ __attribute__((aligned(16))) float lds[L_END];
  const int bid = blockIdx.x, xcd = bid & 7, jj = (bid >> 3) & 3, b = bid >> 5, h = xcd * 4 + jj;
  const int fs_raw = xcd + 8 * jj;
  const bool has_ffn = fs_raw < NFS;
  const int fs = has_ffn ? fs_raw : 0;
  const int tid = threadIdx.x, lane = tid & 63, w = __builtin_amdgcn_readfirstlane(tid >> 6);
  const int g = lane >> 4, c = lane & 15, g8 = lane >> 3, c8 = lane & 7;

  const auto rsK = MKRSRC(kc + (size_t)(b * NH + h) * (SK * DH), SK * DH * 2);
  const auto rsV = MKRSRC(vc + (size_t)(b * NH + h) * (SK * DH), SK * DH * 2);
  const auto rsO = MKRSRC(wo + (size_t)h * DH * DM, DH * DM * 2);
  const auto rs1 = MKRSRC(w1, has_ffn ? DM * FF * 2 : 0);
  const auto rs2 = MKRSRC(w2 + (size_t)fs * 64 * DM, has_ffn ? 64 * DM * 2 : 0);

  const unsigned loff = lane * 16;
  u32x4 SA[4], SB[4];
#define ISSUE(SX, RS, SOFF) do { _Pragma("unroll") for (int u = 0; u < 4; ++u) SX[u] = BLD(RS, loff + u * 1024, (SOFF), KVAUX); FENCE(); } while (0)
  const int wofs = w * 4096;
  ISSUE(SA, rsK, wofs);

  const int rb = (w + 2 * b) & 15;
  const unsigned short* xb = x + b * DM;
  unsigned xr[5];
#pragma unroll
  for (int j = 0; j < 5; ++j) xr[j] = LDG(unsigned, xb, lane * 4 + 256 * j);
  const unsigned xme = LDG(unsigned short, xb + rb * 40, (lane < 40 ? lane : 0) * 2);
  const unsigned b1v = LDG(unsigned short, b1 + fs * 64, lane * 2);
  FENCE();
  u32x4 wqv[10];
  {
    const auto rs = MKRSRC(wq, DM * NH * DH * 2);
    const unsigned voff = (unsigned)(g * 10) * (NH * DH * 2) + c * 16;
    const unsigned soff = (unsigned)(rb * 40) * (NH * DH * 2) + h * (DH * 2);
#pragma unroll
    for (int i = 0; i < 10; ++i) wqv[i] = BLD(rs, voff, soff + i * (NH * DH * 2), WAUX);
  }
  FENCE();

  float sum = 0.f;
#pragma unroll
  for (int j = 0; j < 5; ++j) sum += bflo(xr[j]) + bfhi(xr[j]);
  sum = wave_sum(sum);
  const float mean = sum * (1.0f / DM);
  float sq = 0.f;
#pragma unroll
  for (int j = 0; j < 5; ++j) { const float a = bflo(xr[j]) - mean, bb = bfhi(xr[j]) - mean; sq += a * a + bb * bb; }
  sq = wave_sum(sq);
  const float mb = bfr(mean), vb = bfr(sq * (1.0f / DM));
  const float den = bfr(sqrtf(bfr(vb + 1e-5f)));
  const float xn = bfr(bfr(bflo(xme) - mb) / den);
  float* xs = lds + L_XS + rb * 40;
  if (lane < 40) xs[lane] = xn;
  __builtin_amdgcn_s_waitcnt(0xC07F);
  __builtin_amdgcn_wave_barrier();

  {
    float qa[8];
#pragma unroll
    for (int k = 0; k < 8; ++k) qa[k] = 0.f;
#pragma unroll
    for (int i = 0; i < 10; i += 2) {
      const f32x2 xv = *reinterpret_cast<const f32x2*>(xs + g * 10 + i);
      axpy8(qa, xv[0], wqv[i]); axpy8(qa, xv[1], wqv[i + 1]);
    }
#pragma unroll
    for (int k = 0; k < 8; ++k) qa[k] = xrow_sum(qa[k]);
    if (lane < 16) {
      *reinterpret_cast<f32x4*>(lds + L_RQ + rb * RQS + c * 8) = f32x4{qa[0], qa[1], qa[2], qa[3]};
      *reinterpret_cast<f32x4*>(lds + L_RQ + rb * RQS + c * 8 + 4) = f32x4{qa[4], qa[5], qa[6], qa[7]};
    }
  }
  FENCE();
  ISSUE(SB, rsK, wofs + ST_B);
  __syncthreads();
  if (w < 2) {
    float s = 0.f;
#pragma unroll
    for (int ww = 0; ww < NW; ++ww) s += lds[L_RQ + ww * RQS + tid];
    reinterpret_cast<unsigned short*>(lds + L_Q)[tid] = bfbits(s);
  } else if (w == 3 && lane < 16) {
    lds[L_HV + 64 + lane] = 0.f;
    lds[L_OV + 128 + lane] = 0.f;
  }
  __syncthreads();
  const u32x4 qpk = *reinterpret_cast<const u32x4*>(lds + L_Q + c * 4);

  float mx = -INFINITY;
  float* scw = lds + L_SC + w * 16 + g * 4;
#define QK(SX, ST) do { \
    float sc_[4]; \
    _Pragma("unroll") for (int u = 0; u < 4; ++u) { float d = dot2(SX[u][0], qpk[0], 0.f); d = dot2(SX[u][1], qpk[1], d); d = dot2(SX[u][2], qpk[2], d); d = dot2(SX[u][3], qpk[3], d); sc_[u] = d; } \
    _Pragma("unroll") for (int u = 0; u < 4; ++u) sc_[u] = bfr(bfr(row16_sum(sc_[u])) * 0.088388347648318440f); \
    mx = fmaxf(fmaxf(fmaxf(sc_[0], sc_[1]), fmaxf(sc_[2], sc_[3])), mx); \
    if (c == 0) *reinterpret_cast<f32x4*>(scw + (ST) * 256) = f32x4{sc_[0], sc_[1], sc_[2], sc_[3]}; \
    asm volatile("" : "+v"(mx)); \
    FENCE(); \
  } while (0)
#define STEPBAR() do { __builtin_amdgcn_s_barrier(); FENCE(); } while (0)
#define MIDBAR() STEPBAR()
#define KIT_A(IT) do { QK(SA, 2 * (IT)); ISSUE(SA, rsK, wofs + (2 * (IT) + 2) * ST_B); } while (0)
#define KIT_B(IT) do { MIDBAR(); QK(SB, 2 * (IT) + 1); ISSUE(SB, rsK, wofs + (2 * (IT) + 3) * ST_B); } while (0)

  const int slot = tid / 80;
  u32x4 w1v[5];
  KIT_A(0);
  {
    const unsigned voff = (unsigned)(g8 * 5) * (FF * 2) + c8 * 16;
    const unsigned soff = (unsigned)(rb * 40) * (FF * 2) + fs * 128;
#pragma unroll
    for (int i = 0; i < 5; ++i) w1v[i] = BLD(rs1, voff, soff + i * (FF * 2), WAUX);
    FENCE();
  }
  KIT_B(0);
  STEPBAR();
  KIT_A(1);
  KIT_B(1);
  {
    float ha[8];
#pragma unroll
    for (int k = 0; k < 8; ++k) ha[k] = 0.f;
#pragma unroll
    for (int i = 0; i < 5; ++i) axpy8(ha, xs[g8 * 5 + i], w1v[i]);
#pragma unroll
    for (int k = 0; k < 8; ++k) { float t = ha[k]; t += dpp<ROR8>(t); ha[k] = xrow_sum(t); }
    if (lane < 8) {
      *reinterpret_cast<f32x4*>(lds + L_RH + rb * 64 + c8 * 8) = f32x4{ha[0], ha[1], ha[2], ha[3]};
      *reinterpret_cast<f32x4*>(lds + L_RH + rb * 64 + c8 * 8 + 4) = f32x4{ha[4], ha[5], ha[6], ha[7]};
    }
  }
  __syncthreads(); FENCE();
  if (w == 0) {
    float s = 0.f;
#pragma unroll
    for (int ww = 0; ww < NW; ++ww) s += lds[L_RH + ww * 64 + lane];
    const float h2 = bfr(bfr(s) + bflo(b1v));
    const float inner = 0.7978845608028654f * (h2 + 0.044715f * h2 * h2 * h2);
    lds[L_HV + lane] = bfr(0.5f * h2 * (1.0f + tanhf(inner)));
  }
  FENCE();
  u32x4 w2v[6];
  KIT_A(2);
#pragma unroll
  for (int i = 0; i < 6; ++i) w2v[i] = BLD(rs2, tid * 16, 960 * 16 * i, WAUX);
  FENCE();
  KIT_B(2);
  __syncthreads(); FENCE();
  KIT_A(3);
  KIT_B(3);
  {
    float ya[8];
#pragma unroll
    for (int k = 0; k < 8; ++k) ya[k] = 0.f;
#pragma unroll
    for (int i = 0; i < 6; ++i) axpy8(ya, lds[L_HV + slot + 12 * i], w2v[i]);
    *reinterpret_cast<f32x4*>(lds + L_RED + tid * 8) = f32x4{ya[0], ya[1], ya[2], ya[3]};
    *reinterpret_cast<f32x4*>(lds + L_RED + tid * 8 + 4) = f32x4{ya[4], ya[5], ya[6], ya[7]};
  }
  __syncthreads(); FENCE();
  float ysum = 0.f;
  if (w < 10) {
#pragma unroll
    for (int sl = 0; sl < 12; ++sl) ysum += lds[L_RED + sl * DM + tid];
  }
  FENCE();
  u32x4 wov[11];
#define KIT_WO(IT, W0, W1) do { KIT_A(IT); \
    _Pragma("unroll") for (int i = (W0); i < (W1); ++i) wov[i] = BLD(rsO, tid * 16, 960 * 16 * i, WAUX); \
    FENCE(); KIT_B(IT); STEPBAR(); } while (0)
  KIT_WO(4, 0, 4);
  KIT_WO(5, 4, 8);
  KIT_WO(6, 8, 11);
  QK(SA, 14); ISSUE(SA, rsV, wofs);
  QK(SB, 15); ISSUE(SB, rsV, wofs + ST_B);

  mx = xrow_max(mx);
  if (lane == 0) lds[L_ML + w] = mx;
  __syncthreads();
  {
    const float M = max16(lds + L_ML);
    f32x4 e = *reinterpret_cast<const f32x4*>(lds + L_SC + tid * 4);
#pragma unroll
    for (int k = 0; k < 4; ++k) e[k] = __builtin_amdgcn_exp2f((e[k] - M) * 1.4426950408889634f);
    const float ps = wave_sum((e[0] + e[1]) + (e[2] + e[3]));
    if (lane == 0) lds[L_ML + NW + w] = ps;
    __syncthreads();
    const float inv = 1.0f / sum16(lds + L_ML + NW);
#pragma unroll
    for (int k = 0; k < 4; ++k) e[k] = bfr(e[k] * inv);
    *reinterpret_cast<f32x4*>(lds + L_SC + tid * 4) = e;
  }
  __syncthreads(); FENCE();

  float acc[8];
#pragma unroll
  for (int k = 0; k < 8; ++k) acc[k] = 0.f;
#define PV(SX, ST) do { \
    const f32x4 p_ = *reinterpret_cast<const f32x4*>(scw + (ST) * 256); \
    _Pragma("unroll") for (int u = 0; u < 4; ++u) fma8(acc, p_[u], SX[u]); \
    PIN8(acc); FENCE(); \
  } while (0)
#pragma unroll 1
  for (int it = 0; it < NST / 2 - 1; ++it) {
    PV(SA, 2 * it); ISSUE(SA, rsV, wofs + (2 * it + 2) * ST_B);
    MIDBAR();
    PV(SB, 2 * it + 1); ISSUE(SB, rsV, wofs + (2 * it + 3) * ST_B);
    STEPBAR();
  }
  PV(SA, NST - 2);
  PV(SB, NST - 1);

#pragma unroll
  for (int k = 0; k < 8; ++k) acc[k] = xrow_sum(acc[k]);
  if (lane < 16) {
    *reinterpret_cast<f32x4*>(lds + L_RQ + w * RQS + c * 8) = f32x4{acc[0], acc[1], acc[2], acc[3]};
    *reinterpret_cast<f32x4*>(lds + L_RQ + w * RQS + c * 8 + 4) = f32x4{acc[4], acc[5], acc[6], acc[7]};
  }
  __syncthreads();
  {
    const int d = w * 8 + c8;
    float o = lds[L_RQ + (2 * g8) * RQS + d] + lds[L_RQ + (2 * g8 + 1) * RQS + d];
    o += dpp<ROR8>(o);
    o = xrow_sum(o);
    if (lane < 8) lds[L_OV + d] = bfr(o);
  }
  __syncthreads();
  {
    float sa[8];
#pragma unroll
    for (int k = 0; k < 8; ++k) sa[k] = 0.f;
    float ov[11];
#pragma unroll
    for (int i = 0; i < 11; ++i) ov[i] = lds[L_OV + slot + 12 * i];
    __builtin_amdgcn_sched_barrier(0);
#pragma unroll
    for (int i = 0; i < 11; ++i) axpy8(sa, ov[i], wov[i]);
    *reinterpret_cast<f32x4*>(lds + L_RED + tid * 8) = f32x4{sa[0], sa[1], sa[2], sa[3]};
    *reinterpret_cast<f32x4*>(lds + L_RED + tid * 8 + 4) = f32x4{sa[4], sa[5], sa[6], sa[7]};
    __syncthreads();
    if (w < 10) {
      float s = 0.f;
#pragma unroll
      for (int sl = 0; sl < 12; ++sl) s += lds[L_RED + sl * DM + tid];
      sp[(size_t)(b * NH + h) * DM + tid] = s;
      if (has_ffn) yp[(size_t)(b * NFS + fs) * DM + tid] = ysum;
    }
  }
}

__global__ void __launch_bounds__(256) final_k(const unsigned short* x, const unsigned short* b2, const float* sp,
                                               const float* yp, unsigned short* out) {
  __shared__ float red[2][4][64];
  const int b = blockIdx.x / 10, cb = blockIdx.x % 10;
  const int lane = threadIdx.x & 63, w = threadIdx.x >> 6;
  const int col = cb * 64 + lane;
  float s = 0.f, y = 0.f;
  float sv[8], yv[7];
  const unsigned xv = x[b * DM + col], b2v = b2[col];
#pragma unroll
  for (int i = 0; i < 8; ++i) sv[i] = sp[(size_t)(b * NH + w * 8 + i) * DM + col];
#pragma unroll
  for (int i = 0; i < 7; ++i) { const int f = w * 7 + i; yv[i] = yp[(size_t)(b * NFS + (f < NFS ? f : 0)) * DM + col]; }
  FENCE();
  if (w == 3) yv[6] = 0.f;
#pragma unroll
  for (int i = 0; i < 8; ++i) s += sv[i];
#pragma unroll
  for (int i = 0; i < 7; ++i) y += yv[i];
  red[0][w][lane] = s; red[1][w][lane] = y;
  __syncthreads();
  if (w == 0) {
    s = (red[0][0][lane] + red[0][1][lane]) + (red[0][2][lane] + red[0][3][lane]);
    y = (red[1][0][lane] + red[1][1][lane]) + (red[1][2][lane] + red[1][3][lane]);
    const float sb = bfr(s);
    const float yb = bfr(bfr(y) + bflo(b2v));
    const float gt = bfr(yb / (1.0f + __expf(-yb)));
    const float o = bflo(xv) + bfr(sb * gt);
    out[b * DM + col] = bfbits(o);
  }
}
}

extern "C" void kernel_launch(void* const* d_in, const int* in_sizes, int n_in, void* d_out, int out_size, void* d_ws, size_t ws_size,
                              hipStream_t stream) {
  const unsigned short* x = (const unsigned short*)d_in[0];
  const unsigned short* kc = (const unsigned short*)d_in[1];
  const unsigned short* vc = (const unsigned short*)d_in[2];
  const unsigned short* wq = (const unsigned short*)d_in[3];
  const unsigned short* wo = (const unsigned short*)d_in[4];
  const unsigned short* w1 = (const unsigned short*)d_in[6];
  const unsigned short* b1 = (const unsigned short*)d_in[7];
  const unsigned short* w2 = (const unsigned short*)d_in[8];
  const unsigned short* b2 = (const unsigned short*)d_in[9];
  float* sp = (float*)d_ws;
  float* yp = sp + NB * NH * DM;
  fused_attn<<<NB * NH, NTHR, 0, stream>>>(x, kc, vc, wq, wo, w1, b1, w2, sp, yp);
  final_k<<<NB * 10, 256, 0, stream>>>(x, b2, sp, yp, (unsigned short*)d_out);
}
```
